```python
import torch

def _softmax(x, axis=-1):
    return torch.softmax(x, dim=axis)
def _gelu(x):
    return torch.nn.functional.gelu(x, approximate="tanh")
def _silu(x):
    return torch.nn.functional.silu(x)
def _sigmoid(x):
    return torch.sigmoid(x)
def _layernorm(x, axis=-1, eps=1e-5):
    m = x.mean(dim=axis, keepdim=True)
    v = x.var(dim=axis, keepdim=True, unbiased=False)
    return (x - m) / torch.sqrt(v + eps)
def _scatter_add(tgt, idx, src, axis=0):
    return tgt.clone().index_add_(axis, idx, src)
def _segment_sum(src, seg, n):
    out = torch.zeros((n,) + src.shape[1:], dtype=src.dtype)
    return out.index_add_(0, seg, src)
def _pad(x, pw):
    flat = tuple(v for pair in reversed(pw) for v in pair)
    return torch.nn.functional.pad(x, flat)

kernel_name = 'standalone__glu_gated_swiglu__sampling'

def setup_inputs():

    _w = (lambda a: (lambda t: t.to(torch.bfloat16) if t.is_floating_point() else t)(torch.as_tensor(a)))
    B, S, D, F = 32, 32, 512, 1792
    return {
        "x": _w((lambda *s: torch.randn(*s, dtype=torch.bfloat16) * 0.05)(B, S, D)),
        "w1": _w((lambda *s: torch.randn(*s, dtype=torch.bfloat16) * 0.05)(D, F) * (2.0 / D) ** 0.5),
        "w3": _w((lambda *s: torch.randn(*s, dtype=torch.bfloat16) * 0.05)(D, F) * (2.0 / D) ** 0.5),
        "w2": _w((lambda *s: torch.randn(*s, dtype=torch.bfloat16) * 0.05)(F, D) * (2.0 / F) ** 0.5),
    }

def reference(x, w1, w3, w2):
    gate = torch.einsum("bsd,df->bsf", x, w1)
    up = torch.einsum("bsd,df->bsf", x, w3)
    h = _silu(gate) * up
    out = torch.einsum("bsf,fd->bsd", h, w2)
    return out
```

```cpp
#include <hip/hip_runtime.h>
#include <hip/hip_bf16.h>
typedef __attribute__((ext_vector_type(8))) short bf16x8;
typedef __attribute__((ext_vector_type(4))) float f32x4;
typedef unsigned short u16;
constexpr int M=1024, D=512, F=1792;

__device__ __forceinline__ float bf2f(u16 v){ return __uint_as_float(((unsigned)v)<<16); }
__device__ __forceinline__ u16 f2bf(float f){ __hip_bfloat16 b = __float2bfloat16(f); return *(u16*)&b; }

__global__ void k1(const u16* x, const u16* w1, const u16* w3, u16* h){
  int lane = threadIdx.x & 63; int wave = (blockIdx.x*blockDim.x + threadIdx.x)>>6;
  int tm = wave / (F/16), tn = wave % (F/16);
  int r = lane&15, q = lane>>4;
  f32x4 g={0,0,0,0}, u={0,0,0,0};
  for(int k0=0;k0<D;k0+=32){
    bf16x8 a = *(const bf16x8*)(x + (tm*16+r)*D + k0 + q*8);
    bf16x8 b1,b3;
    for(int j=0;j<8;j++){ b1[j]=w1[(k0+q*8+j)*F + tn*16 + r]; b3[j]=w3[(k0+q*8+j)*F + tn*16+r]; }
    g = __builtin_amdgcn_mfma_f32_16x16x32_bf16(a,b1,g,0,0,0);
    u = __builtin_amdgcn_mfma_f32_16x16x32_bf16(a,b3,u,0,0,0);
  }
  for(int j=0;j<4;j++){
    float gg=g[j]; float s = gg/(1.f+__expf(-gg));
    h[(tm*16+q*4+j)*F + tn*16 + r] = f2bf(s*u[j]);
  }
}
__global__ void k2(const u16* h, const u16* w2, u16* out){
  int lane = threadIdx.x & 63; int wave = (blockIdx.x*blockDim.x + threadIdx.x)>>6;
  int tm = wave / (D/16), tn = wave % (D/16);
  int r = lane&15, q = lane>>4;
  f32x4 c={0,0,0,0};
  for(int k0=0;k0<F;k0+=32){
    bf16x8 a = *(const bf16x8*)(h + (tm*16+r)*F + k0 + q*8);
    bf16x8 b;
    for(int j=0;j<8;j++){ b[j]=w2[(k0+q*8+j)*D + tn*16 + r]; }
    c = __builtin_amdgcn_mfma_f32_16x16x32_bf16(a,b,c,0,0,0);
  }
  for(int j=0;j<4;j++) out[(tm*16+q*4+j)*D + tn*16 + r] = f2bf(c[j]);
}
extern "C" void kernel_launch(void* const* d_in, const int* in_sizes, int n_in,
                              void* d_out, int out_size, void* d_ws, size_t ws_size,
                              hipStream_t stream) {
  const u16* x=(const u16*)d_in[0]; const u16* w1=(const u16*)d_in[1]; const u16* w3=(const u16*)d_in[2]; const u16* w2=(const u16*)d_in[3];
  u16* h=(u16*)d_ws; u16* out=(u16*)d_out;
  k1<<<(M/16)*(F/16)/4,256,0,stream>>>(x,w1,w3,h);
  k2<<<(M/16)*(D/16)/4,256,0,stream>>>(h,w2,out);
}
```

```cpp
#include <hip/hip_runtime.h>
#include <hip/hip_bf16.h>

typedef unsigned short u16;
typedef unsigned long long u64;
typedef __attribute__((ext_vector_type(8))) short bf16x8;
typedef __attribute__((ext_vector_type(4))) short s16x4;
typedef __attribute__((ext_vector_type(16))) float f32x16;
typedef __attribute__((ext_vector_type(4))) float f32x4;
typedef __attribute__((ext_vector_type(4))) unsigned v4u;
typedef __attribute__((ext_vector_type(2))) unsigned v2u;
typedef __attribute__((ext_vector_type(2))) __bf16 bf16x2;
typedef __attribute__((ext_vector_type(2))) float f32x2;
#define LAS __attribute__((address_space(3)))
#define GAS __attribute__((address_space(1)))
typedef GAS u64 gu64;
#define RLX_AGENT __ATOMIC_RELAXED, __HIP_MEMORY_SCOPE_AGENT

constexpr int M = 1024, D = 512, F = 1792;
constexpr int SLOT = 32768;
constexpr int LDS_BYTES = 160 * 1024;
constexpr size_t WS_E = 0;
constexpr size_t WS_ACK = 4096;
constexpr size_t WS_FLAG = 8192;
constexpr size_t WS_H = 1 << 20;
constexpr unsigned SPIN_CAP = 1u << 17;
#define LOOKJ 3
#define MAP2 1

#define STAMP(i) do {} while (0)
#define CSTAMP(i) do {} while (0)

__device__ __forceinline__ void dma16(unsigned m0v, unsigned voff, __amdgpu_buffer_rsrc_t rs, unsigned soff) {
  asm volatile("s_mov_b32 m0, %0\n\ts_nop 0\n\tbuffer_load_dwordx4 %1, %2, %3 offen lds" ::"s"(m0v), "v"(voff), "s"(rs), "s"(soff) : "memory");
}
template <int N> __device__ __forceinline__ void wait_vm() { asm volatile("s_waitcnt vmcnt(%0)" ::"n"(N) : "memory"); }
__device__ __forceinline__ void wait_lgkm0() { asm volatile("s_waitcnt lgkmcnt(0)" ::: "memory"); }
__device__ __forceinline__ void bar() { asm volatile("" ::: "memory"); __builtin_amdgcn_s_barrier(); asm volatile("" ::: "memory"); }
__device__ __forceinline__ bf16x8 ld_b128(unsigned a) { return *(LAS bf16x8*)a; }
__device__ __forceinline__ s16x4 ld_tr(unsigned a) { return __builtin_amdgcn_ds_read_tr16_b64_v4i16((LAS s16x4*)a); }
__device__ __forceinline__ bf16x8 cat(s16x4 a, s16x4 b) { return __builtin_shufflevector(a, b, 0, 1, 2, 3, 4, 5, 6, 7); }
__device__ __forceinline__ unsigned pk_bf16(float a, float b) {
  f32x2 v = {a, b};
  bf16x2 r = __builtin_convertvector(v, bf16x2);
  return __builtin_bit_cast(unsigned, r);
}
__device__ __forceinline__ float swiglu(float g, float u) {
  float e = __builtin_amdgcn_exp2f(g * -1.44269504088896340736f);
  return g * u * __builtin_amdgcn_rcpf(1.0f + e);
}
__device__ __forceinline__ u64 mix_tag(u64 e) { e ^= e >> 31; e *= 0x9E3779B97F4A7C15ull; e += 0xD1B54A32D192ED03ull; return e; }

__global__ __launch_bounds__(512, 2) void swiglu_fused(const u16* __restrict__ x, const u16* __restrict__ w1, const u16* __restrict__ w3,
                                                       const u16* __restrict__ w2, u16* __restrict__ out, char* __restrict__ ws
) {
  extern __shared__ __attribute__((aligned(16))) char smem[];
  const int tid = threadIdx.x, lane = tid & 63;
  const int wave = __builtin_amdgcn_readfirstlane(tid >> 6);
  const int b = blockIdx.x, xcd = b & 7, r = b >> 3;
  STAMP(0); CSTAMP(14);
  const bool prod = r < 28;
  const int mi = r & 3, fi = r >> 2;
  const int mt = (xcd & 1) * 4 + mi, ft = (xcd >> 1) * 7 + fi;
  const int rot = (2 * mi + fi) & 7;
  const int tt = (xcd >> 1) * 8 + (r & 7), ds = (xcd & 1) * 4 + (r >> 3), rot2 = (r + 2 * (r >> 3)) & 7;
  const unsigned lds0 = (unsigned)(size_t)(LAS char*)smem;

  const __amdgpu_buffer_rsrc_t rs_h = __builtin_amdgcn_make_buffer_rsrc(ws + WS_H, 0, M * F * 2, 0x00020000);
  const int hh = lane >> 5, blk = (lane >> 4) & 1, q = (lane & 15) >> 2, p = lane & 3;
  const int kr = (wave + rot2) & 7;
  bf16x8 wf2[14][2];
  const unsigned tr2 = (4 * hh + q) * 128 + (((2 * blk + (p >> 1)) ^ (4 * ((q >> 1) & 1))) * 16) + 8 * (p & 1);
  u64 tag;

  if (wave >= 4) {
    const int lw = wave - 4;
    u64 ev;
    asm volatile("global_load_dwordx2 %0, %1, %2 sc1" : "=v"(ev) : "v"(0u), "s"(ws + WS_E) : "memory");
    const __amdgpu_buffer_rsrc_t rs_x = __builtin_amdgcn_make_buffer_rsrc((void*)x, 0, M * D * 2, 0x00020000);
    const __amdgpu_buffer_rsrc_t rs_1 = __builtin_amdgcn_make_buffer_rsrc((void*)w1, 0, D * F * 2, 0x00020000);
    const __amdgpu_buffer_rsrc_t rs_3 = __builtin_amdgcn_make_buffer_rsrc((void*)w3, 0, D * F * 2, 0x00020000);
    const __amdgpu_buffer_rsrc_t rs_2 = __builtin_amdgcn_make_buffer_rsrc((void*)w2, 0, D * F * 2, 0x00020000);
    const unsigned vx = (lane >> 3) * 1024 + (((lane & 7) ^ (4 * (lw & 1) + (lane >> 4))) * 16);
    const unsigned sw = ((lane & 7) ^ (((lane >> 4) & 1) * 4)) * 16;
    const unsigned vw = (lane >> 3) * (F * 2) + sw;
    const unsigned v2 = (lane >> 3) * (D * 2) + sw;
    const unsigned sx0 = (mt * 128 + 8 * lw) * 1024, sw0 = (8 * lw) * (F * 2) + ft * 128;
    auto issue = [&](int T, int n) {
      if (T < 8) {
        if (prod) {
          const int kt = (T + rot) & 7;
          const unsigned sl = lds0 + (T & 3) * SLOT;
          if (n < 4) dma16(sl + (lw + 4 * n) * 1024, vx, rs_x, sx0 + n * 32768 + kt * 128);
          else dma16(sl + 16384 + ((n - 4) >> 1) * 8192 + (lw + 4 * (n & 1)) * 1024, vw, n < 6 ? rs_1 : rs_3, sw0 + (kt * 64 + 32 * (n & 1)) * (F * 2));
        }
      } else {
        int slot, P;
        const int krp = (lw + rot2) & 7;
        if (T < 10) { slot = T == 8 ? 4 : 0; P = 28 * kr + (T - 8) * 8 + n; }
        else {
          const int n2 = (T - 10) * 8 + n;
          slot = T == 10 ? 1 : T == 11 ? 2 : T == 12 ? 4 : T == 13 ? 0 : 3;
          P = n2 < 28 ? 28 * krp + n2 : 28 * kr + 16 + (n2 - 28);
        }
        dma16(lds0 + slot * SLOT + lw * 8192 + n * 1024, v2, rs_2, P * 8192 + ds * 128);
      }
    };
#pragma unroll
    for (int n = 0; n < 16 + LOOKJ; ++n) issue(n >> 3, n & 7);
#pragma unroll
    for (int k = 0; k < 8; ++k) {
      if (prod || k == 0) wait_vm<8 + LOOKJ>();
      if (k == 0 && !prod) wait_vm<0>();
      bar();
#pragma unroll
      for (int n = 0; n < 8; ++n) { const int g = (k + 2) * 8 + LOOKJ + n; issue(g >> 3, g & 7); }
    }
    STAMP(1);
    asm volatile("" : "+v"(ev));
    tag = mix_tag(((u64)__builtin_amdgcn_readfirstlane((unsigned)(ev >> 32)) << 32) | (unsigned)__builtin_amdgcn_readfirstlane((unsigned)ev));
#pragma unroll
    for (int g = 80 + LOOKJ; g < 96; ++g) issue(g >> 3, g & 7);
    wait_vm<16>();
    STAMP(2);
#pragma unroll
    for (int i = 0; i < 8; ++i) {
      const unsigned a = lds0 + (i < 4 ? 4 : 0) * SLOT + lw * 8192 + (i & 3) * 2048 + tr2;
      wf2[i][0] = cat(ld_tr(a), ld_tr(a + 1024));
      wf2[i][1] = cat(ld_tr(a ^ 64), ld_tr((a ^ 64) + 1024));
    }
    wait_lgkm0();
#pragma unroll
    for (int i = 0; i < 8; ++i) asm volatile("" : "+v"(wf2[i][0]), "+v"(wf2[i][1]));
#pragma unroll
    for (int g = 96; g < 112; ++g) issue(g >> 3, g & 7);
    bar();
#pragma unroll
    for (int g = 112; g < 120; ++g) issue(g >> 3, g & 7);
    STAMP(3);
    wait_vm<0>();
    bar();
    STAMP(4);
#pragma unroll
    for (int i = 8; i < 14; ++i) {
      const int n2 = 28 + (2 * i - 16);
      const int T = 10 + (n2 >> 3), slot = T == 10 ? 1 : T == 11 ? 2 : T == 12 ? 4 : T == 13 ? 0 : 3;
      const unsigned a = lds0 + slot * SLOT + lw * 8192 + (n2 & 7) * 1024 + tr2;
      wf2[i][0] = cat(ld_tr(a), ld_tr(a + 1024));
      wf2[i][1] = cat(ld_tr(a ^ 64), ld_tr((a ^ 64) + 1024));
    }
  } else {
    const int wm = wave & 1, wf = wave >> 1;
    f32x16 ag[2] = {}, au[2] = {};
    const unsigned l31 = lane & 31, sx = (l31 >> 1) & 7;
    unsigned xa[4];
#pragma unroll
    for (int s = 0; s < 4; ++s) xa[s] = lds0 + (64 * wm + l31) * 128 + (((2 * s + hh) ^ sx) * 16);
    const unsigned wa = lds0 + 16384 + (8 * hh + q) * 128 + (((4 * wf + 2 * blk + (p >> 1)) ^ (4 * ((q >> 1) & 1))) * 16) + 8 * (p & 1);
    u64 ev;
    asm volatile("global_load_dwordx2 %0, %1, %2 sc1\n\ts_waitcnt vmcnt(0)" : "=&v"(ev) : "v"(0u), "s"(ws + WS_E) : "memory");
    tag = mix_tag(((u64)__builtin_amdgcn_readfirstlane((unsigned)(ev >> 32)) << 32) | (unsigned)__builtin_amdgcn_readfirstlane((unsigned)ev));
    bar();
    STAMP(1); CSTAMP(9);
    if (wave == 0 && lane == 0) __hip_atomic_store((gu64*)(ws + WS_ACK) + b, tag, RLX_AGENT);
    if (prod) {
      bf16x8 X[2][2], G[2], U[2];
      unsigned xh[4], wh = wa + 65536;
#pragma unroll
      for (int s = 0; s < 4; ++s) { xh[s] = xa[s] + 65536; asm volatile("" : "+v"(xa[s]), "+v"(xh[s])); }
      unsigned wl = wa;
      asm volatile("" : "+v"(wl), "+v"(wh));
      auto ldf = [&](int buf, int slot, int s) {
        const unsigned o = (slot & 1) * SLOT;
        const unsigned xb = slot < 2 ? xa[s] : xh[s], wb = slot < 2 ? wl : wh;
        G[buf] = cat(ld_tr(wb + o + s * 2048), ld_tr(wb + o + s * 2048 + 512));
        X[buf][0] = ld_b128(xb + o);
        U[buf] = cat(ld_tr(wb + o + s * 2048 + 8192), ld_tr(wb + o + s * 2048 + 8192 + 512));
        X[buf][1] = ld_b128(xb + o + 4096);
      };
      ldf(0, 0, 0);
#pragma unroll
      for (int k = 0; k < 8; ++k) {
#pragma unroll
        for (int s = 0; s < 4; ++s) {
          const int cur = s & 1;
          __builtin_amdgcn_sched_barrier(0);
          if (s == 3 && k < 7) { wait_lgkm0(); bar(); }
          if (s < 3) ldf(cur ^ 1, k & 3, s + 1);
          else if (k < 7) ldf(cur ^ 1, (k + 1) & 3, 0);
          ag[0] = __builtin_amdgcn_mfma_f32_32x32x16_bf16(G[cur], X[cur][0], ag[0], 0, 0, 0);
          au[0] = __builtin_amdgcn_mfma_f32_32x32x16_bf16(U[cur], X[cur][0], au[0], 0, 0, 0);
          ag[1] = __builtin_amdgcn_mfma_f32_32x32x16_bf16(G[cur], X[cur][1], ag[1], 0, 0, 0);
          au[1] = __builtin_amdgcn_mfma_f32_32x32x16_bf16(U[cur], X[cur][1], au[1], 0, 0, 0);
          if (s < 3 || k < 7) {
            __builtin_amdgcn_sched_group_barrier(0x8, 1, 0);
            __builtin_amdgcn_sched_group_barrier(0x100, 2, 0);
            __builtin_amdgcn_sched_group_barrier(0x8, 1, 0);
            __builtin_amdgcn_sched_group_barrier(0x100, 2, 0);
            __builtin_amdgcn_sched_group_barrier(0x8, 1, 0);
            __builtin_amdgcn_sched_group_barrier(0x100, 2, 0);
            __builtin_amdgcn_sched_group_barrier(0x8, 1, 0);
          }
        }
      }
      __builtin_amdgcn_sched_barrier(0);
      STAMP(2); CSTAMP(10);
#pragma unroll
      for (int tb = 0; tb < 2; ++tb)
#pragma unroll
        for (int s2 = 0; s2 < 2; ++s2) {
          v4u hv;
#pragma unroll
          for (int j = 0; j < 4; ++j)
            hv[j] = pk_bf16(swiglu(ag[tb][8 * s2 + 2 * j], au[tb][8 * s2 + 2 * j]), swiglu(ag[tb][8 * s2 + 2 * j + 1], au[tb][8 * s2 + 2 * j + 1]));
          __builtin_amdgcn_raw_buffer_store_b128(hv, rs_h, lane * 16, ((4 * mt + 2 * wm + tb) * 112 + 4 * ft + 2 * wf + s2) * 1024, 16);
        }
      STAMP(3); CSTAMP(11);
      wait_vm<0>();
      if (lane == 0) __hip_atomic_store((gu64*)(ws + WS_FLAG + ((mt * 28 + ft) * 4 + wave) * 128), tag, RLX_AGENT);
      STAMP(4);
    } else {
#pragma unroll
      for (int k = 1; k < 8; ++k) bar();
      if (b == 255 && wave == 0) {
        gu64* ack = (gu64*)(ws + WS_ACK) + lane;
        for (unsigned spins = 0; spins < SPIN_CAP; ++spins) {
          const u64 a0 = __hip_atomic_load(ack, RLX_AGENT), a1 = __hip_atomic_load(ack + 64, RLX_AGENT), a2 = __hip_atomic_load(ack + 128, RLX_AGENT),
                    a3 = __hip_atomic_load(ack + 192, RLX_AGENT);
          if (__all(a0 == tag && a1 == tag && a2 == tag && a3 == tag)) break;
          __builtin_amdgcn_s_sleep(4);
        }
        if (lane == 0) __hip_atomic_store((gu64*)(ws + WS_E), tag, RLX_AGENT);
      }
    }
    bar();
    bar();
    STAMP(5);
#pragma unroll
    for (int i = 0; i < 14; ++i) {
      const int n2 = 2 * i;
      const int T = 10 + (n2 >> 3), slot = T == 10 ? 1 : T == 11 ? 2 : T == 12 ? 4 : T == 13 ? 0 : 3;
      const unsigned a = lds0 + slot * SLOT + wave * 8192 + (n2 & 7) * 1024 + tr2;
      wf2[i][0] = cat(ld_tr(a), ld_tr(a + 1024));
      wf2[i][1] = cat(ld_tr(a ^ 64), ld_tr((a ^ 64) + 1024));
    }
  }
  {
    const int j = 7 * kr + (lane < 7 ? lane : 0);
    gu64* pf = (gu64*)(ws + WS_FLAG + (((tt >> 2) * 28 + (j >> 1)) * 4 + ((tt >> 1) & 1) + 2 * (j & 1)) * 128);
    for (unsigned spins = 0; spins < SPIN_CAP; ++spins) {
      if (__all(__hip_atomic_load(pf, RLX_AGENT) == tag)) break;
    }
    asm volatile("" ::: "memory");
  }
  STAMP(6); CSTAMP(12);
  v4u hf[14];
#pragma unroll
  for (int i = 0; i < 14; ++i) hf[i] = __builtin_amdgcn_raw_buffer_load_b128(rs_h, lane * 16, (tt * 112 + 14 * kr + i) * 1024, 16);
  __builtin_amdgcn_sched_barrier(0);
  wait_lgkm0();
  bar();
  __builtin_amdgcn_sched_barrier(0);
  f32x16 acc[2] = {};
#pragma unroll
  for (int i = 0; i < 14; ++i) {
    const bf16x8 hb = __builtin_bit_cast(bf16x8, hf[i]);
    acc[0] = __builtin_amdgcn_mfma_f32_32x32x16_bf16(wf2[i][0], hb, acc[0], 0, 0, 0);
    acc[1] = __builtin_amdgcn_mfma_f32_32x32x16_bf16(wf2[i][1], hb, acc[1], 0, 0, 0);
  }
  STAMP(7); CSTAMP(13);
  {
    const unsigned t = lane & 31;
    const unsigned base = lds0 + wave * 8192 + t * 256;
#pragma unroll
    for (int dh = 0; dh < 2; ++dh)
#pragma unroll
      for (int g = 0; g < 4; ++g) {
        const f32x4 v = {acc[dh][4 * g], acc[dh][4 * g + 1], acc[dh][4 * g + 2], acc[dh][4 * g + 3]};
        *(LAS f32x4*)(base + (((8 * dh + 2 * g + hh) ^ (t & 7)) * 16)) = v;
      }
    wait_lgkm0();
    bar();
    const unsigned t2 = tid >> 4, c = tid & 15;
    const unsigned ra = lds0 + t2 * 256 + ((c ^ (t2 & 7)) * 16);
    f32x4 s = *(LAS f32x4*)ra;
#pragma unroll
    for (int w = 1; w < 8; ++w) s += *(LAS f32x4*)(ra + w * 8192);
    v2u o = {pk_bf16(s[0], s[1]), pk_bf16(s[2], s[3])};
    *(v2u*)(out + (size_t)(tt * 32 + t2) * D + ds * 64 + c * 4) = o;
  }
  STAMP(8); CSTAMP(15);
}

extern "C" void kernel_launch(void* const* d_in, const int* in_sizes, int n_in, void* d_out, int out_size, void* d_ws, size_t ws_size,
                              hipStream_t stream) {
  (void)hipFuncSetAttribute((const void*)swiglu_fused, hipFuncAttributeMaxDynamicSharedMemorySize, LDS_BYTES);
  swiglu_fused<<<256, 512, LDS_BYTES, stream>>>((const u16*)d_in[0], (const u16*)d_in[1], (const u16*)d_in[2], (const u16*)d_in[3], (u16*)d_out,
                                                (char*)d_ws);
}
```

```cpp
#include <hip/hip_runtime.h>

typedef unsigned short u16;
typedef unsigned long long u64;
typedef __attribute__((ext_vector_type(8))) short bf16x8;
typedef __attribute__((ext_vector_type(4))) short s16x4;
typedef __attribute__((ext_vector_type(4))) float f32x4;
typedef __attribute__((ext_vector_type(4))) unsigned v4u;
typedef __attribute__((ext_vector_type(2))) unsigned v2u;
typedef __attribute__((ext_vector_type(2))) __bf16 bf16x2;
typedef __attribute__((ext_vector_type(2))) float f32x2;
#define LAS __attribute__((address_space(3)))
#define GAS __attribute__((address_space(1)))
typedef GAS u64 gu64;
#define RLX_AGENT __ATOMIC_RELAXED, __HIP_MEMORY_SCOPE_AGENT

constexpr int M = 1024, D = 512, F = 1792;
constexpr int SLOT = 32768;
constexpr int LDS_BYTES = 160 * 1024;
constexpr unsigned WS_E = 0;
constexpr unsigned WS_ACK = 4096;
constexpr unsigned WS_FLAG = 16384;
constexpr unsigned WS_H = 1 << 20;
constexpr unsigned HT = 56 * 1024;
constexpr unsigned SPIN_CAP = 1u << 16;
constexpr unsigned CODE_BYTES = 13312;
constexpr int SC1 = 16;
#define SB() __builtin_amdgcn_sched_barrier(0)
#define SGB(m, n) __builtin_amdgcn_sched_group_barrier(m, n, 0)

__device__ __forceinline__ void dma16(unsigned m0v, unsigned voff, __amdgpu_buffer_rsrc_t rs, unsigned soff) {
  asm volatile("s_mov_b32 m0, %0\n\ts_nop 0\n\tbuffer_load_dwordx4 %1, %2, %3 offen lds" ::"s"(m0v), "v"(voff), "s"(rs), "s"(soff) : "memory");
}
template <int N> __device__ __forceinline__ void wait_vm() { asm volatile("s_waitcnt vmcnt(%0)" ::"n"(N) : "memory"); }
__device__ __forceinline__ void wait_lgkm0() { asm volatile("s_waitcnt lgkmcnt(0)" ::: "memory"); }
__device__ __forceinline__ void bar() { asm volatile("" ::: "memory"); __builtin_amdgcn_s_barrier(); asm volatile("" ::: "memory"); }
__device__ __forceinline__ bf16x8 ld_b128(unsigned a) { return *(LAS bf16x8*)(size_t)a; }
__device__ __forceinline__ s16x4 ld_tr(unsigned a) { return __builtin_amdgcn_ds_read_tr16_b64_v4i16((LAS s16x4*)(size_t)a); }
__device__ __forceinline__ bf16x8 cat(s16x4 a, s16x4 b) { return __builtin_shufflevector(a, b, 0, 1, 2, 3, 4, 5, 6, 7); }
__device__ __forceinline__ unsigned pk_bf16(f32x2 v) { return __builtin_bit_cast(unsigned, __builtin_convertvector(v, bf16x2)); }
__device__ __forceinline__ u64 mix_tag(u64 e) { e ^= e >> 31; e *= 0x9E3779B97F4A7C15ull; e += 0xD1B54A32D192ED03ull; return e; }
__device__ __forceinline__ u64 uni64(u64 v) {
  return ((u64)(unsigned)__builtin_amdgcn_readfirstlane((unsigned)(v >> 32)) << 32) | (unsigned)__builtin_amdgcn_readfirstlane((unsigned)v);
}
__device__ __forceinline__ f32x4 mfma(bf16x8 a, bf16x8 b, f32x4 c) { return __builtin_amdgcn_mfma_f32_16x16x32_bf16(a, b, c, 0, 0, 0); }
__device__ __forceinline__ __amdgpu_buffer_rsrc_t mk_rsrc(const void* p, unsigned bytes) {
  return __builtin_amdgcn_make_buffer_rsrc((void*)p, 0, bytes, 0x00020000);
}

__global__ __launch_bounds__(512, 2) void swiglu_fused(const u16* __restrict__ x, const u16* __restrict__ w1, const u16* __restrict__ w3,
                                                       const u16* __restrict__ w2, u16* __restrict__ out, char* __restrict__ ws) {
  extern __shared__ __attribute__((aligned(16))) char smem[];
  const int tid = threadIdx.x, lane = tid & 63;
  const int wave = __builtin_amdgcn_readfirstlane(tid >> 6);
  const int b = blockIdx.x, xcd = b & 7, r = b >> 3;
  u64 pc0;
  asm volatile("s_getpc_b64 %0" : "=s"(pc0));
  const bool prod = r < 28;
  const int mi = r & 3, fi = r >> 2;
  const int mt = (xcd & 1) * 4 + mi, ft = (xcd >> 1) * 7 + fi;
  const int tt = xcd * 4 + mi, ds = fi == 7 ? 3 : fi == 3 ? 7 : fi;
  const int rot3 = fi % 7;
  const int lw = wave - 4;
  unsigned vx = (lane >> 3) * 1024 + (((lane & 7) ^ (4 * (lw & 1) + (lane >> 4))) * 16);
  unsigned vw = (lane >> 3) * (F * 2) + (((lane & 7) ^ ((((lane >> 4) & 1) + 2 * (lw & 1)) << 1)) * 16);
  unsigned sx0 = (mt * 128 + 8 * lw) * 1024, sw0 = (8 * lw) * (F * 2) + ft * 128, rot = 4 * ((mi + fi) & 1), lwk = lw * 1024;
  asm volatile("" : "+v"(vx), "+v"(vw));
  asm volatile("" : "+s"(sx0), "+s"(sw0), "+s"(rot), "+s"(lwk));
  asm volatile("" ::"s"(x), "s"(w1), "s"(w3), "s"(w2), "s"(out), "s"(ws));
  if (__builtin_expect(wave >= 4 && prod, 1)) {
    const __amdgpu_buffer_rsrc_t rs_x = mk_rsrc(x, M * D * 2), rs_1 = mk_rsrc(w1, D * F * 2), rs_3 = mk_rsrc(w3, D * F * 2);
#pragma unroll
    for (int n = 0; n < 4; ++n) dma16(lwk + 4096 * n, vx, rs_x, sx0 + n * 32768 + rot * 128);
#pragma unroll
    for (int n = 0; n < 4; ++n) dma16(lwk + 16384 + (n >> 1) * 8192 + 4096 * (n & 1), vw, n < 2 ? rs_1 : rs_3, sw0 + (rot * 64 + 32 * (n & 1)) * (F * 2));
  }

  const __amdgpu_buffer_rsrc_t rs_h = mk_rsrc(ws + WS_H, M * F * 2);
  const unsigned hl = lane * 16;
  const int g4 = lane >> 4, q = (lane & 15) >> 2, p = lane & 3;
  bf16x8 wf2[7][4];
  const unsigned tr2 = (4 * g4 + q) * 128 + (((p >> 1) ^ (((2 * g4 + (q >> 1)) & 3) << 1)) * 16) + 8 * (p & 1);
  auto ks_of = [&](int w, int s) { int t = s + rot3; t = t >= 7 ? t - 7 : t; return 7 * w + t; };
  auto rd_w2 = [&](int s, unsigned a) {
#pragma unroll
    for (int db = 0; db < 4; ++db) { const unsigned a2 = a + (tr2 ^ (db * 32)); wf2[s][db] = cat(ld_tr(a2), ld_tr(a2 + 2048)); }
  };
  v4u hf[7][2];
  auto ld_h = [&](int s) {
#pragma unroll
    for (int th = 0; th < 2; ++th) { hf[s][th] = __builtin_amdgcn_raw_buffer_load_b128(rs_h, hl + th * HT, (2 * tt * 56 + ks_of(wave, s)) * 1024, SC1); SB(); }
  };
  f32x4 acc[2][4] = {};
  unsigned rw[4], ra, oo;
  auto red_addr = [&]() {
#pragma unroll
    for (int db = 0; db < 4; ++db) rw[db] = 3 * SLOT + (wave ^ 4) * 8192 + (lane & 15) * 256 + (((4 * db + g4) ^ (lane & 7)) * 16);
    ra = 3 * SLOT + (tid >> 4) * 256 + (((tid & 15) ^ ((tid >> 4) & 7)) * 16);
    oo = (tid >> 4) * (D * 2) + (tid & 15) * 8;
    asm volatile("" : "+v"(rw[0]), "+v"(rw[1]), "+v"(rw[2]), "+v"(rw[3]), "+v"(ra), "+v"(oo));
  };
  auto p2_step = [&](int s) {
#pragma unroll
    for (int th = 0; th < 2; ++th) {
      const bf16x8 hb = __builtin_bit_cast(bf16x8, hf[s][th]);
#pragma unroll
      for (int db = 0; db < 4; ++db) acc[th][db] = mfma(wf2[s][db], hb, acc[th][db]);
    }
  };

  if (__builtin_expect(wave >= 4, 1)) {
    const __amdgpu_buffer_rsrc_t rs_2 = mk_rsrc(w2, D * F * 2);
    const unsigned v2 = (lane >> 3) * (D * 2) + (((lane & 7) ^ (((lane >> 4) & 3) << 1)) * 16);
    auto issue2 = [&](int n) {
      const int T = n >> 3, slot = T == 0 || T == 4 ? 4 : T == 1 || T == 5 ? 0 : T == 2 || T == 6 ? 1 : 2;
      const int m = n < 28 ? n : n - 28;
      const int P = 4 * ks_of(n < 28 ? wave : lw, m >> 2) + (m & 3);
      dma16(slot * SLOT + lw * 8192 + (n & 7) * 1024, v2, rs_2, P * 8192 + ds * 128);
    };
    if (__builtin_expect(prod, 1)) {
      const __amdgpu_buffer_rsrc_t rs_x = mk_rsrc(x, M * D * 2), rs_1 = mk_rsrc(w1, D * F * 2), rs_3 = mk_rsrc(w3, D * F * 2);
      auto issue = [&](int T) {
        const int kt = (T + rot) & 7;
        const unsigned sl = (T & 3) * SLOT;
#pragma unroll
        for (int n = 0; n < 4; ++n) dma16(sl + (lw + 4 * n) * 1024, vx, rs_x, sx0 + n * 32768 + kt * 128);
#pragma unroll
        for (int n = 0; n < 4; ++n) dma16(sl + 16384 + (n >> 1) * 8192 + (lw + 4 * (n & 1)) * 1024, vw, n < 2 ? rs_1 : rs_3, sw0 + (kt * 64 + 32 * (n & 1)) * (F * 2));
      };
      issue(1);
#pragma unroll
      for (int k = 0; k < 8; ++k) {
        if (k == 2 || k == 3) wait_vm<10>(); else wait_vm<8>();
        bar();
        if (k < 6) issue(k + 2);
        else {
#pragma unroll
          for (int n = 0; n < 8; ++n) issue2((k - 6) * 8 + n);
        }
        if (k == 1) {
          dma16(4 * SLOT + lw * 8192, lane * 16, rs_2, (b * 4 + lw) * 2048);
          dma16(4 * SLOT + lw * 8192 + 1024, lane * 16, rs_2, (b * 4 + lw) * 2048 + 1024);
        }
      }
    } else {
      dma16(3 * SLOT + lw * 8192, lane * 16, mk_rsrc((const void*)(pc0 & ~255ull), CODE_BYTES), (mi * 4 + lw) * 1024);
#pragma unroll
      for (int n = 0; n < 16; ++n) issue2(n);
    }
#pragma unroll
    for (int n = 16; n < 24; ++n) issue2(n);
#pragma unroll
    for (int T = 0; T < 4; ++T) {
      wait_vm<16>();
      const unsigned a = (T == 0 ? 4 : T - 1) * SLOT + lw * 8192;
      rd_w2(2 * T, a);
      if (T < 3) rd_w2(2 * T + 1, a + 4096);
      SB();
#pragma unroll
      for (int n = 24 + 8 * T; n < 32 + 8 * T; ++n) issue2(n);
      wait_lgkm0();
#pragma unroll
      for (int i = 0; i < 4; ++i) { asm volatile("" : "+v"(wf2[2 * T][i])); if (T < 3) asm volatile("" : "+v"(wf2[2 * T + 1][i])); }
    }
    red_addr();
    wait_vm<0>();
    bar();
#pragma unroll
    for (int s = 0; s < 4; ++s) ld_h(s);
#pragma unroll
    for (int s = 0; s < 7; ++s) {
      p2_step(s); SB();
      if (s + 4 < 7) ld_h(s + 4);
    }
  } else {
    u64 tag;
    if (__builtin_expect(prod, 1)) {
      u64 ev;
      asm volatile("global_load_dwordx2 %0, %1, %2 sc1" : "=v"(ev) : "v"(0u), "s"(ws + WS_E) : "memory");
      const int wm = wave & 1, wf = wave >> 1;
      const unsigned hs = ((8 * mt + 4 * wm) * 56 + 2 * ft + wf) * 1024;
      const unsigned myflag = WS_FLAG + ((mt * 2 + wm) * 56 + 2 * ft + wf) * 128;
      f32x4 ag[4][2] = {}, au[4][2] = {};
      const unsigned l15 = lane & 15, sx = l15 >> 1;
      unsigned xa[2][2], wa[2][2];
#pragma unroll
      for (int s = 0; s < 2; ++s) {
        xa[0][s] = (64 * wm + l15) * 128 + (((4 * s + g4) ^ sx) * 16);
        wa[0][s] = 16384 + (8 * g4 + q) * 128 + (((4 * wf + 2 * s + (p >> 1)) ^ ((((q >> 1) & 1) + 2 * (g4 & 1)) << 1)) * 16) + 8 * (p & 1);
        xa[1][s] = xa[0][s] + 65536; wa[1][s] = wa[0][s] + 65536;
        asm volatile("" : "+v"(xa[0][s]), "+v"(xa[1][s]), "+v"(wa[0][s]), "+v"(wa[1][s]));
      }
      bar();
      bf16x8 X[3][4], G[3][2], U[3][2];
      auto ldf = [&](int buf, int slot, int s) {
        const unsigned o = (slot & 1) * SLOT;
        const int h = slot >> 1;
#pragma unroll
        for (int fb = 0; fb < 2; ++fb) G[buf][fb] = cat(ld_tr(wa[h][fb] + o + s * 4096), ld_tr(wa[h][fb] + o + s * 4096 + 512));
        X[buf][0] = ld_b128(xa[h][s] + o);
#pragma unroll
        for (int fb = 0; fb < 2; ++fb) U[buf][fb] = cat(ld_tr(wa[h][fb] + o + s * 4096 + 8192), ld_tr(wa[h][fb] + o + s * 4096 + 8192 + 512));
#pragma unroll
        for (int tb = 1; tb < 4; ++tb) X[buf][tb] = ld_b128(xa[h][s] + o + tb * 2048);
      };
      auto mm = [&](int buf, int tb) {
        ag[tb][0] = mfma(G[buf][0], X[buf][tb], ag[tb][0]); ag[tb][1] = mfma(G[buf][1], X[buf][tb], ag[tb][1]);
        au[tb][0] = mfma(U[buf][0], X[buf][tb], au[tb][0]); au[tb][1] = mfma(U[buf][1], X[buf][tb], au[tb][1]);
      };
      ldf(0, 0, 0);
#pragma unroll
      for (int k = 0; k < 7; ++k) {
        SB();
        ldf(1, k & 3, 1);
#pragma unroll
        for (int tb = 0; tb < 4; ++tb) mm(0, tb);
#pragma unroll
        for (int i = 0; i < 12; ++i) { SGB(0x8, 1); SGB(0x100, 1); }
        SGB(0x8, 4);
        SB();
        if (k == 2) asm volatile("s_waitcnt vmcnt(0)" : "+v"(ev)::"memory");
        wait_lgkm0(); bar();
        if (k == 2) {
          tag = mix_tag(uni64(ev));
          if (wave == 0 && lane == 0) __hip_atomic_store((gu64*)(ws + WS_ACK + b * 8), tag, RLX_AGENT);
        }
        SB();
        ldf(0, (k + 1) & 3, 0);
        if (k == 6) ldf(2, 3, 1);
#pragma unroll
        for (int tb = 0; tb < 4; ++tb) mm(1, tb);
        if (k < 6) {
#pragma unroll
          for (int i = 0; i < 12; ++i) { SGB(0x8, 1); SGB(0x100, 1); }
          SGB(0x8, 4);
        } else {
#pragma unroll
          for (int i = 0; i < 8; ++i) { SGB(0x8, 1); SGB(0x100, 2); SGB(0x8, 1); SGB(0x100, 1); }
        }
      }
      SB();
      mm(0, 0); mm(2, 0);
#pragma unroll
      for (int tb = 0; tb < 4; ++tb) {
        const int n = tb + 1;
        auto nx = [&](int i) {
          SB();
          if (n < 4) {
            const int buf = i < 4 ? 0 : 2, fb = i & 1;
            if (i & 2) au[n][fb] = mfma(U[buf][fb], X[buf][n], au[n][fb]); else ag[n][fb] = mfma(G[buf][fb], X[buf][n], ag[n][fb]);
          }
        };
        f32x2 g[4], u[4], t[4];
#pragma unroll
        for (int j = 0; j < 4; ++j) {
          g[j] = f32x2{ag[tb][j >> 1][2 * (j & 1)], ag[tb][j >> 1][2 * (j & 1) + 1]};
          u[j] = f32x2{au[tb][j >> 1][2 * (j & 1)], au[tb][j >> 1][2 * (j & 1) + 1]};
        }
        SB();
#pragma unroll
        for (int j = 0; j < 4; ++j) t[j] = g[j] * -1.44269504088896340736f;
#pragma unroll
        for (int j = 0; j < 8; ++j) { SB(); t[j >> 1][j & 1] = __builtin_amdgcn_exp2f(t[j >> 1][j & 1]); if (j < 4) nx(j); }
        SB();
#pragma unroll
        for (int j = 0; j < 4; ++j) t[j] = t[j] + 1.0f;
#pragma unroll
        for (int j = 0; j < 8; ++j) { SB(); t[j >> 1][j & 1] = __builtin_amdgcn_rcpf(t[j >> 1][j & 1]); if (j < 4) nx(4 + j); }
        SB();
#pragma unroll
        for (int j = 0; j < 4; ++j) t[j] = g[j] * t[j];
        SB();
#pragma unroll
        for (int j = 0; j < 4; ++j) u[j] = u[j] * t[j];
        SB();
        v4u hv;
#pragma unroll
        for (int j = 0; j < 4; ++j) hv[j] = pk_bf16(u[j]);
        __builtin_amdgcn_raw_buffer_store_b128(hv, rs_h, hl, hs + tb * HT, SC1);
        asm volatile("s_nop 1" ::"v"(hv));
      }
      wait_vm<0>();
      if (lane == 0) __hip_atomic_store((gu64*)(ws + myflag), tag, RLX_AGENT);
      __builtin_amdgcn_s_sleep(16);
    } else {
      const u64 t0 = __builtin_amdgcn_s_memrealtime();
      u64 ev;
      asm volatile("global_load_dwordx2 %0, %1, %2 sc1\n\ts_waitcnt vmcnt(0)" : "=&v"(ev) : "v"(0u), "s"(ws + WS_E) : "memory");
      tag = mix_tag(uni64(ev));
      if (lane == 0) __hip_atomic_store((gu64*)(ws + WS_ACK + (224 + (b - 224) * 4 + wave) * 8), tag, RLX_AGENT);
      if (wave == 0) {
        while (__builtin_amdgcn_s_memrealtime() < t0 + 500) __builtin_amdgcn_s_sleep(2);
      }
      if (b == 255 && wave == 1) {
        while (__builtin_amdgcn_s_memrealtime() < t0 + 350) __builtin_amdgcn_s_sleep(8);
        gu64* ack = (gu64*)(ws + WS_ACK) + lane;
        for (unsigned spins = 0; spins < SPIN_CAP; ++spins) {
          const u64 a0 = __hip_atomic_load(ack, RLX_AGENT), a1 = __hip_atomic_load(ack + 64, RLX_AGENT), a2 = __hip_atomic_load(ack + 128, RLX_AGENT),
                    a3 = __hip_atomic_load(ack + 192, RLX_AGENT), a4 = __hip_atomic_load(ack + 256, RLX_AGENT),
                    a5 = __hip_atomic_load(ack + (lane < 32 ? 320 : 256), RLX_AGENT);
          if (__all(a0 == tag && a1 == tag && a2 == tag && a3 == tag && a4 == tag && a5 == tag)) break;
          __builtin_amdgcn_s_sleep(4);
        }
        if (lane == 0) __hip_atomic_store((gu64*)(ws + WS_E), tag, RLX_AGENT);
      }
    }
    red_addr();
    if (wave == 0) {
      gu64* pf = (gu64*)(ws + WS_FLAG + ((tt >> 1) * 56 + (lane < 56 ? lane : 0)) * 128);
      for (unsigned spins = 0; spins < SPIN_CAP; ++spins) {
        if (__all(__hip_atomic_load(pf, RLX_AGENT) == tag)) break;
        if (!prod) __builtin_amdgcn_s_sleep(4);
      }
    }
    bar();
    auto rd_p = [&](int s) {
      const int n = 28 + 4 * s, T = n >> 3, slot = T == 3 ? 2 : T == 4 ? 4 : T == 5 ? 0 : 1;
      rd_w2(s, slot * SLOT + wave * 8192 + (n & 7) * 1024);
    };
#pragma unroll
    for (int s = 0; s < 4; ++s) ld_h(s);
    rd_p(0); rd_p(1);
#pragma unroll
    for (int s = 0; s < 7; ++s) {
      SB();
      if (s < 5) rd_p(s + 2);
      p2_step(s);
      if (s < 5) {
#pragma unroll
        for (int i = 0; i < 8; ++i) { SGB(0x8, 1); SGB(0x100, 1); }
      }
      SB();
      if (s + 4 < 7) ld_h(s + 4);
    }
  }
  SB();
  {
#pragma unroll
    for (int th = 0; th < 2; ++th)
#pragma unroll
      for (int db = 0; db < 4; ++db) *(LAS f32x4*)(size_t)(rw[db] + th * 4096) = acc[th][db];
    wait_lgkm0();
    bar();
    f32x4 pt[8];
#pragma unroll
    for (int w = 0; w < 8; ++w) pt[w] = *(LAS f32x4*)(size_t)(ra + w * 8192);
    SB();
    const f32x4 s = ((pt[0] + pt[1]) + (pt[2] + pt[3])) + ((pt[4] + pt[5]) + (pt[6] + pt[7]));
    const v2u o = {pk_bf16(f32x2{s[0], s[1]}), pk_bf16(f32x2{s[2], s[3]})};
    __builtin_amdgcn_raw_buffer_store_b64(o, mk_rsrc(out, M * D * 2), oo, tt * 32 * (D * 2) + ds * 128, SC1);
  }
}

extern "C" void kernel_launch(void* const* d_in, const int* in_sizes, int n_in, void* d_out, int out_size, void* d_ws, size_t ws_size,
                              hipStream_t stream) {
  (void)hipFuncSetAttribute((const void*)swiglu_fused, hipFuncAttributeMaxDynamicSharedMemorySize, LDS_BYTES);
  swiglu_fused<<<256, 512, LDS_BYTES, stream>>>((const u16*)d_in[0], (const u16*)d_in[1], (const u16*)d_in[2], (const u16*)d_in[3], (u16*)d_out,
                                                (char*)d_ws);
}
```

```cpp
#include <hip/hip_runtime.h>

typedef unsigned short u16;
typedef unsigned long long u64;
typedef __attribute__((ext_vector_type(8))) short bf16x8;
typedef __attribute__((ext_vector_type(4))) short s16x4;
typedef __attribute__((ext_vector_type(4))) float f32x4;
typedef __attribute__((ext_vector_type(4))) unsigned v4u;
typedef __attribute__((ext_vector_type(2))) unsigned v2u;
typedef __attribute__((ext_vector_type(2))) __bf16 bf16x2;
typedef __attribute__((ext_vector_type(2))) float f32x2;
#define LAS __attribute__((address_space(3)))
#define GAS __attribute__((address_space(1)))
typedef GAS u64 gu64;
#define RLX_AGENT __ATOMIC_RELAXED, __HIP_MEMORY_SCOPE_AGENT

constexpr int M = 1024, D = 512, F = 1792;
constexpr int SLOT = 32768;
constexpr int LDS_BYTES = 160 * 1024;
constexpr unsigned WS_E = 0;
constexpr unsigned WS_ACK = 4096;
constexpr unsigned WS_FLAG = 16384;
constexpr unsigned WS_E2 = WS_FLAG + 896 * 128;
constexpr unsigned WS_H = 1 << 20;
constexpr unsigned HT = 56 * 1024;
constexpr unsigned SPIN_CAP = 1u << 16;
constexpr unsigned CODE_BYTES = 13312;
constexpr int SC1 = 16;
#define SB() __builtin_amdgcn_sched_barrier(0)
#define SGB(m, n) __builtin_amdgcn_sched_group_barrier(m, n, 0)

__device__ __forceinline__ void dma16(unsigned m0v, unsigned voff, __amdgpu_buffer_rsrc_t rs, unsigned soff) {
  asm volatile("s_mov_b32 m0, %0\n\ts_nop 0\n\tbuffer_load_dwordx4 %1, %2, %3 offen lds" ::"s"(m0v), "v"(voff), "s"(rs), "s"(soff) : "memory");
}
template <int N> __device__ __forceinline__ void wait_vm() { asm volatile("s_waitcnt vmcnt(%0)" ::"n"(N) : "memory"); }
__device__ __forceinline__ void wait_lgkm0() { asm volatile("s_waitcnt lgkmcnt(0)" ::: "memory"); }
__device__ __forceinline__ void bar() { asm volatile("" ::: "memory"); __builtin_amdgcn_s_barrier(); asm volatile("" ::: "memory"); }
__device__ __forceinline__ bf16x8 ld_b128(unsigned a) { return *(LAS bf16x8*)(size_t)a; }
__device__ __forceinline__ s16x4 ld_tr(unsigned a) { return __builtin_amdgcn_ds_read_tr16_b64_v4i16((LAS s16x4*)(size_t)a); }
__device__ __forceinline__ bf16x8 cat(s16x4 a, s16x4 b) { return __builtin_shufflevector(a, b, 0, 1, 2, 3, 4, 5, 6, 7); }
__device__ __forceinline__ unsigned pk_bf16(f32x2 v) { return __builtin_bit_cast(unsigned, __builtin_convertvector(v, bf16x2)); }
__device__ __forceinline__ u64 make_tag(u64 v) {
  const unsigned lo = (unsigned)v, hi = (unsigned)(v >> 32);
  const u64 e1 = ((u64)(unsigned)__builtin_amdgcn_readlane(hi, 0) << 32) | (unsigned)__builtin_amdgcn_readlane(lo, 0);
  const u64 e2 = ((u64)(unsigned)__builtin_amdgcn_readlane(lo, 1) << 32) | (unsigned)__builtin_amdgcn_readlane(hi, 1);
  u64 e = e1 + 3 * e2;
  e ^= e >> 31; e *= 0x9E3779B97F4A7C15ull; e += 0xD1B54A32D192ED03ull;
  return e;
}
__device__ __forceinline__ f32x4 mfma(bf16x8 a, bf16x8 b, f32x4 c) { return __builtin_amdgcn_mfma_f32_16x16x32_bf16(a, b, c, 0, 0, 0); }
__device__ __forceinline__ __amdgpu_buffer_rsrc_t mk_rsrc(const void* p, unsigned bytes) {
  return __builtin_amdgcn_make_buffer_rsrc((void*)p, 0, bytes, 0x00020000);
}

__global__ __launch_bounds__(512, 2) void swiglu_fused(const u16* __restrict__ x, const u16* __restrict__ w1, const u16* __restrict__ w3,
                                                       const u16* __restrict__ w2, u16* __restrict__ out, char* __restrict__ ws) {
  extern __shared__ __attribute__((aligned(16))) char smem[];
  const int tid = threadIdx.x, lane = tid & 63;
  const int wave = __builtin_amdgcn_readfirstlane(tid >> 6);
  const int b = blockIdx.x, xcd = b & 7, r = b >> 3;
  u64 pc0;
  asm volatile("s_getpc_b64 %0" : "=s"(pc0));
  const bool prod = r < 28;
  const int mi = r & 3, fi = r >> 2;
  const int mt = (xcd & 1) * 4 + mi, ft = (xcd >> 1) * 7 + fi;
  const int tt = xcd * 4 + mi, ds = fi == 7 ? 3 : fi == 3 ? 7 : fi;
  const int rot3 = fi % 7;
  const int lw = wave - 4;
  unsigned vx = (lane >> 3) * 1024 + (((lane & 7) ^ (4 * (lw & 1) + (lane >> 4))) * 16);
  unsigned vw = (lane >> 3) * (F * 2) + (((lane & 7) ^ ((((lane >> 4) & 1) + 2 * (lw & 1)) << 1)) * 16);
  unsigned sx0 = (mt * 128 + 8 * lw) * 1024, sw0 = (8 * lw) * (F * 2) + ft * 128, rot = (4 * ((mi + fi) & 1) + 2 * (xcd & 1)) & 7, lwk = lw * 1024;
  asm volatile("" : "+v"(vx), "+v"(vw));
  asm volatile("" : "+s"(sx0), "+s"(sw0), "+s"(rot), "+s"(lwk));
  asm volatile("" ::"s"(x), "s"(w1), "s"(w3), "s"(w2), "s"(out), "s"(ws));
  if (__builtin_expect(wave >= 4 && prod, 1)) {
    const __amdgpu_buffer_rsrc_t rs_x = mk_rsrc(x, M * D * 2), rs_1 = mk_rsrc(w1, D * F * 2), rs_3 = mk_rsrc(w3, D * F * 2);
#pragma unroll
    for (int n = 0; n < 4; ++n) dma16(lwk + 4096 * n, vx, rs_x, sx0 + n * 32768 + rot * 128);
#pragma unroll
    for (int n = 0; n < 4; ++n) dma16(lwk + 16384 + (n >> 1) * 8192 + 4096 * (n & 1), vw, n < 2 ? rs_1 : rs_3, sw0 + (rot * 64 + 32 * (n & 1)) * (F * 2));
  }

  const __amdgpu_buffer_rsrc_t rs_h = mk_rsrc(ws + WS_H, M * F * 2);
  const unsigned hl = lane * 16;
  const int g4 = lane >> 4, q = (lane & 15) >> 2, p = lane & 3;
  bf16x8 wf2[7][4];
  const unsigned tr2 = (4 * g4 + q) * 128 + (((p >> 1) ^ (((2 * g4 + (q >> 1)) & 3) << 1)) * 16) + 8 * (p & 1);
  auto ks_of = [&](int w, int s) { int t = s + rot3; t = t >= 7 ? t - 7 : t; return 7 * w + t; };
  auto rd_w2 = [&](int s, unsigned a) {
#pragma unroll
    for (int db = 0; db < 4; ++db) { const unsigned a2 = a + (tr2 ^ (db * 32)); wf2[s][db] = cat(ld_tr(a2), ld_tr(a2 + 2048)); }
  };
  v4u hf[7][2];
  auto ld_h = [&](int s) {
#pragma unroll
    for (int th = 0; th < 2; ++th) { hf[s][th] = __builtin_amdgcn_raw_buffer_load_b128(rs_h, hl + th * HT, (2 * tt * 56 + ks_of(wave, s)) * 1024, SC1); SB(); }
  };
  f32x4 acc[2][4] = {};
  unsigned rw[4], ra, oo;
  auto red_addr = [&]() {
#pragma unroll
    for (int db = 0; db < 4; ++db) rw[db] = 3 * SLOT + (wave ^ 4) * 8192 + (lane & 15) * 256 + (((4 * db + g4) ^ (lane & 7)) * 16);
    ra = 3 * SLOT + (tid >> 4) * 256 + (((tid & 15) ^ ((tid >> 4) & 7)) * 16);
    oo = (tid >> 4) * (D * 2) + (tid & 15) * 8;
    asm volatile("" : "+v"(rw[0]), "+v"(rw[1]), "+v"(rw[2]), "+v"(rw[3]), "+v"(ra), "+v"(oo));
  };
  auto p2_step = [&](int s) {
#pragma unroll
    for (int th = 0; th < 2; ++th) {
      const bf16x8 hb = __builtin_bit_cast(bf16x8, hf[s][th]);
#pragma unroll
      for (int db = 0; db < 4; ++db) acc[th][db] = mfma(wf2[s][db], hb, acc[th][db]);
    }
  };

  auto spare_time = [&]() {
    const int W = mi * 8 + wave, j7 = (xcd >> 1) * 7, fa = (3 - j7) & 3;
    const int f0 = j7 + fa + 4 * (W >> 4);
    const unsigned dst = 3 * SLOT + wave * 4096;
    if (f0 < j7 + 7) {
      const __amdgpu_buffer_rsrc_t rs = mk_rsrc((W >> 3) & 1 ? w3 : w1, D * F * 2);
#pragma unroll
      for (int u = 2; u < 4; ++u)
        dma16(dst + (u & 1) * 1024, (lane >> 3) * (4 * F) + (lane & 7) * 16, rs, (64 * ((u + (W & 4) + 2 * (xcd & 1)) & 7) + ((f0 >> 2) & 1) + 16 * (W & 3)) * (F * 2) + f0 * 128);
    }
    const __amdgpu_buffer_rsrc_t rs = mk_rsrc(w2, D * F * 2);
    dma16(dst + 2048, lane * 16, rs, ((((fa * 4 + mi) * 8 + xcd) * 8) + wave) * 1024);
    if (fa + 4 < 7) dma16(dst + 3072, lane * 16, rs, (((((fa + 4) * 4 + mi) * 8 + xcd) * 8) + wave) * 1024);
  };
  if (__builtin_expect(wave >= 4, 1)) {
    const __amdgpu_buffer_rsrc_t rs_2 = mk_rsrc(w2, D * F * 2);
    const unsigned v2 = (lane >> 3) * (D * 2) + (((lane & 7) ^ (((lane >> 4) & 3) << 1)) * 16);
    auto issue2 = [&](int n) {
      const int T = n >> 3, slot = T == 0 || T == 4 ? 4 : T == 1 || T == 5 ? 0 : T == 2 || T == 6 ? 1 : 2;
      const int m = n < 28 ? n : n - 28;
      const int P = 4 * ks_of(n < 28 ? wave : lw, m >> 2) + (m & 3);
      dma16(slot * SLOT + lw * 8192 + (n & 7) * 1024, v2, rs_2, P * 8192 + ds * 128);
    };
    if (__builtin_expect(prod, 1)) {
      const __amdgpu_buffer_rsrc_t rs_x = mk_rsrc(x, M * D * 2), rs_1 = mk_rsrc(w1, D * F * 2), rs_3 = mk_rsrc(w3, D * F * 2);
      auto issue = [&](int T) {
        const int kt = (T + rot) & 7;
        const unsigned sl = (T & 3) * SLOT;
#pragma unroll
        for (int n = 0; n < 4; ++n) dma16(sl + (lw + 4 * n) * 1024, vx, rs_x, sx0 + n * 32768 + kt * 128);
#pragma unroll
        for (int n = 0; n < 4; ++n) dma16(sl + 16384 + (n >> 1) * 8192 + (lw + 4 * (n & 1)) * 1024, vw, n < 2 ? rs_1 : rs_3, sw0 + (kt * 64 + 32 * (n & 1)) * (F * 2));
      };
      issue(1);
      const bool dopf = (ft & 3) != 3;
#pragma unroll
      for (int k = 0; k < 8; ++k) {
        if ((k == 2 || k == 3) && dopf) wait_vm<10>(); else wait_vm<8>();
        bar();
        if (k < 6) issue(k + 2);
        else {
#pragma unroll
          for (int n = 0; n < 8; ++n) issue2((k - 6) * 8 + n);
        }
        if (k == 1 && dopf) {
          dma16(4 * SLOT + lw * 8192, lane * 16, rs_2, (b * 4 + lw) * 2048);
          dma16(4 * SLOT + lw * 8192 + 1024, lane * 16, rs_2, (b * 4 + lw) * 2048 + 1024);
        }
      }
    } else {
      spare_time();
      dma16(3 * SLOT + wave * 4096, lane * 16, mk_rsrc((const void*)(pc0 & ~255ull), CODE_BYTES), (mi * 4 + lw) * 1024);
#pragma unroll
      for (int n = 0; n < 16; ++n) issue2(n);
    }
#pragma unroll
    for (int n = 16; n < 24; ++n) issue2(n);
#pragma unroll
    for (int T = 0; T < 4; ++T) {
      wait_vm<16>();
      const unsigned a = (T == 0 ? 4 : T - 1) * SLOT + lw * 8192;
      rd_w2(2 * T, a);
      if (T < 3) rd_w2(2 * T + 1, a + 4096);
      SB();
#pragma unroll
      for (int n = 24 + 8 * T; n < 32 + 8 * T; ++n) issue2(n);
      wait_lgkm0();
#pragma unroll
      for (int i = 0; i < 4; ++i) { asm volatile("" : "+v"(wf2[2 * T][i])); if (T < 3) asm volatile("" : "+v"(wf2[2 * T + 1][i])); }
    }
    red_addr();
    wait_vm<0>();
    bar();
#pragma unroll
    for (int s = 0; s < 4; ++s) ld_h(s);
#pragma unroll
    for (int s = 0; s < 7; ++s) {
      p2_step(s); SB();
      if (s + 4 < 7) ld_h(s + 4);
    }
  } else {
    u64 tag;
    if (__builtin_expect(prod, 1)) {
      u64 ev;
      asm volatile("global_load_dwordx2 %0, %1, %2 sc1" : "=v"(ev) : "v"(lane == 1 ? WS_E2 : WS_E), "s"(ws) : "memory");
      const int wm = wave & 1, wf = wave >> 1;
      const unsigned hs = ((8 * mt + 4 * wm) * 56 + 2 * ft + wf) * 1024;
      const unsigned myflag = WS_FLAG + ((mt * 2 + wm) * 56 + 2 * ft + wf) * 128;
      f32x4 ag[4][2] = {}, au[4][2] = {};
      const unsigned l15 = lane & 15, sx = l15 >> 1;
      unsigned xa[2][2], wa[2][2];
#pragma unroll
      for (int s = 0; s < 2; ++s) {
        xa[0][s] = (64 * wm + l15) * 128 + (((4 * s + g4) ^ sx) * 16);
        wa[0][s] = 16384 + (8 * g4 + q) * 128 + (((4 * wf + 2 * s + (p >> 1)) ^ ((((q >> 1) & 1) + 2 * (g4 & 1)) << 1)) * 16) + 8 * (p & 1);
        xa[1][s] = xa[0][s] + 65536; wa[1][s] = wa[0][s] + 65536;
        asm volatile("" : "+v"(xa[0][s]), "+v"(xa[1][s]), "+v"(wa[0][s]), "+v"(wa[1][s]));
      }
      bar();
      bf16x8 X[3][4], G[3][2], U[3][2];
      auto ldf = [&](int buf, int slot, int s) {
        const unsigned o = (slot & 1) * SLOT;
        const int h = slot >> 1;
#pragma unroll
        for (int fb = 0; fb < 2; ++fb) G[buf][fb] = cat(ld_tr(wa[h][fb] + o + s * 4096), ld_tr(wa[h][fb] + o + s * 4096 + 512));
        X[buf][0] = ld_b128(xa[h][s] + o);
#pragma unroll
        for (int fb = 0; fb < 2; ++fb) U[buf][fb] = cat(ld_tr(wa[h][fb] + o + s * 4096 + 8192), ld_tr(wa[h][fb] + o + s * 4096 + 8192 + 512));
#pragma unroll
        for (int tb = 1; tb < 4; ++tb) X[buf][tb] = ld_b128(xa[h][s] + o + tb * 2048);
      };
      auto mm = [&](int buf, int tb) {
        ag[tb][0] = mfma(G[buf][0], X[buf][tb], ag[tb][0]); ag[tb][1] = mfma(G[buf][1], X[buf][tb], ag[tb][1]);
        au[tb][0] = mfma(U[buf][0], X[buf][tb], au[tb][0]); au[tb][1] = mfma(U[buf][1], X[buf][tb], au[tb][1]);
      };
      ldf(0, 0, 0);
#pragma unroll
      for (int k = 0; k < 7; ++k) {
        SB();
        ldf(1, k & 3, 1);
#pragma unroll
        for (int tb = 0; tb < 4; ++tb) mm(0, tb);
#pragma unroll
        for (int i = 0; i < 12; ++i) { SGB(0x8, 1); SGB(0x100, 1); }
        SGB(0x8, 4);
        SB();
        if (k == 2) asm volatile("s_waitcnt vmcnt(0)" : "+v"(ev)::"memory");
        wait_lgkm0(); bar();
        if (k == 2) {
          tag = make_tag(ev);
          if (wave == 0 && lane == 0) __hip_atomic_store((gu64*)(ws + WS_ACK + b * 8), tag, RLX_AGENT);
        }
        SB();
        ldf(0, (k + 1) & 3, 0);
        if (k == 6) ldf(2, 3, 1);
#pragma unroll
        for (int tb = 0; tb < 4; ++tb) mm(1, tb);
        if (k < 6) {
#pragma unroll
          for (int i = 0; i < 12; ++i) { SGB(0x8, 1); SGB(0x100, 1); }
          SGB(0x8, 4);
        } else {
#pragma unroll
          for (int i = 0; i < 8; ++i) { SGB(0x8, 1); SGB(0x100, 2); SGB(0x8, 1); SGB(0x100, 1); }
        }
      }
      SB();
      mm(0, 0); mm(2, 0);
#pragma unroll
      for (int tb = 0; tb < 4; ++tb) {
        const int n = tb + 1;
        auto nx = [&](int i) {
          SB();
          if (n < 4) {
            const int buf = i < 4 ? 0 : 2, fb = i & 1;
            if (i & 2) au[n][fb] = mfma(U[buf][fb], X[buf][n], au[n][fb]); else ag[n][fb] = mfma(G[buf][fb], X[buf][n], ag[n][fb]);
          }
        };
        f32x2 g[4], u[4], t[4];
#pragma unroll
        for (int j = 0; j < 4; ++j) {
          g[j] = f32x2{ag[tb][j >> 1][2 * (j & 1)], ag[tb][j >> 1][2 * (j & 1) + 1]};
          u[j] = f32x2{au[tb][j >> 1][2 * (j & 1)], au[tb][j >> 1][2 * (j & 1) + 1]};
        }
        SB();
#pragma unroll
        for (int j = 0; j < 4; ++j) t[j] = g[j] * -1.44269504088896340736f;
#pragma unroll
        for (int j = 0; j < 8; ++j) { SB(); t[j >> 1][j & 1] = __builtin_amdgcn_exp2f(t[j >> 1][j & 1]); if (j < 4) nx(j); }
        SB();
#pragma unroll
        for (int j = 0; j < 4; ++j) t[j] = t[j] + 1.0f;
#pragma unroll
        for (int j = 0; j < 8; ++j) { SB(); t[j >> 1][j & 1] = __builtin_amdgcn_rcpf(t[j >> 1][j & 1]); if (j < 4) nx(4 + j); }
        SB();
#pragma unroll
        for (int j = 0; j < 4; ++j) t[j] = g[j] * t[j];
        SB();
#pragma unroll
        for (int j = 0; j < 4; ++j) u[j] = u[j] * t[j];
        SB();
        v4u hv;
#pragma unroll
        for (int j = 0; j < 4; ++j) hv[j] = pk_bf16(u[j]);
        __builtin_amdgcn_raw_buffer_store_b128(hv, rs_h, hl, hs + tb * HT, SC1);
        asm volatile("s_nop 1" ::"v"(hv));
      }
      wait_vm<0>();
      if (lane == 0) __hip_atomic_store((gu64*)(ws + myflag), tag, RLX_AGENT);
      __builtin_amdgcn_s_sleep(16);
    } else {
      const u64 t0 = __builtin_amdgcn_s_memrealtime();
      spare_time();
      u64 ev;
      asm volatile("global_load_dwordx2 %0, %1, %2 sc1\n\ts_waitcnt vmcnt(0)" : "=&v"(ev) : "v"(lane == 1 ? WS_E2 : WS_E), "s"(ws) : "memory");
      tag = make_tag(ev);
      if (lane == 0) __hip_atomic_store((gu64*)(ws + WS_ACK + (224 + (b - 224) * 4 + wave) * 8), tag, RLX_AGENT);
      if (wave == 0) {
        while (__builtin_amdgcn_s_memrealtime() < t0 + 500) __builtin_amdgcn_s_sleep(2);
      }
      if (b == 255 && wave == 1) {
        while (__builtin_amdgcn_s_memrealtime() < t0 + 350) __builtin_amdgcn_s_sleep(8);
        gu64* ack = (gu64*)(ws + WS_ACK) + lane;
        for (unsigned spins = 0; spins < SPIN_CAP; ++spins) {
          const u64 a0 = __hip_atomic_load(ack, RLX_AGENT), a1 = __hip_atomic_load(ack + 64, RLX_AGENT), a2 = __hip_atomic_load(ack + 128, RLX_AGENT),
                    a3 = __hip_atomic_load(ack + 192, RLX_AGENT), a4 = __hip_atomic_load(ack + 256, RLX_AGENT),
                    a5 = __hip_atomic_load(ack + (lane < 32 ? 320 : 256), RLX_AGENT);
          if (__all(a0 == tag && a1 == tag && a2 == tag && a3 == tag && a4 == tag && a5 == tag)) break;
          __builtin_amdgcn_s_sleep(4);
        }
        if (lane < 2) __hip_atomic_store((gu64*)(ws + (lane ? WS_E2 : WS_E)), tag, RLX_AGENT);
      }
    }
    red_addr();
    if (wave == 0) {
      gu64* pf = (gu64*)(ws + WS_FLAG + ((tt >> 1) * 56 + (lane < 56 ? lane : 0)) * 128);
      for (unsigned spins = 0; spins < SPIN_CAP; ++spins) {
        if (__all(__hip_atomic_load(pf, RLX_AGENT) == tag)) break;
        if (!prod) __builtin_amdgcn_s_sleep(4);
      }
    }
    bar();
    auto rd_p = [&](int s) {
      const int n = 28 + 4 * s, T = n >> 3, slot = T == 3 ? 2 : T == 4 ? 4 : T == 5 ? 0 : 1;
      rd_w2(s, slot * SLOT + wave * 8192 + (n & 7) * 1024);
    };
#pragma unroll
    for (int s = 0; s < 4; ++s) ld_h(s);
    rd_p(0); rd_p(1);
#pragma unroll
    for (int s = 0; s < 7; ++s) {
      SB();
      if (s < 5) rd_p(s + 2);
      p2_step(s);
      if (s < 5) {
#pragma unroll
        for (int i = 0; i < 8; ++i) { SGB(0x8, 1); SGB(0x100, 1); }
      }
      SB();
      if (s + 4 < 7) ld_h(s + 4);
    }
  }
  SB();
  {
#pragma unroll
    for (int th = 0; th < 2; ++th)
#pragma unroll
      for (int db = 0; db < 4; ++db) *(LAS f32x4*)(size_t)(rw[db] + th * 4096) = acc[th][db];
    wait_lgkm0();
    bar();
    f32x4 pt[8];
#pragma unroll
    for (int w = 0; w < 8; ++w) pt[w] = *(LAS f32x4*)(size_t)(ra + w * 8192);
    SB();
    const f32x4 s = ((pt[0] + pt[1]) + (pt[2] + pt[3])) + ((pt[4] + pt[5]) + (pt[6] + pt[7]));
    const v2u o = {pk_bf16(f32x2{s[0], s[1]}), pk_bf16(f32x2{s[2], s[3]})};
    __builtin_amdgcn_raw_buffer_store_b64(o, mk_rsrc(out, M * D * 2), oo, tt * 32 * (D * 2) + ds * 128, SC1);
  }
}

extern "C" void kernel_launch(void* const* d_in, const int* in_sizes, int n_in, void* d_out, int out_size, void* d_ws, size_t ws_size,
                              hipStream_t stream) {
  (void)hipFuncSetAttribute((const void*)swiglu_fused, hipFuncAttributeMaxDynamicSharedMemorySize, LDS_BYTES);
  swiglu_fused<<<256, 512, LDS_BYTES, stream>>>((const u16*)d_in[0], (const u16*)d_in[1], (const u16*)d_in[2], (const u16*)d_in[3], (u16*)d_out,
                                                (char*)d_ws);
}
```
